```python
import torch

def _softmax(x, axis=-1):
    return torch.softmax(x, dim=axis)
def _gelu(x):
    return torch.nn.functional.gelu(x, approximate="tanh")
def _silu(x):
    return torch.nn.functional.silu(x)
def _sigmoid(x):
    return torch.sigmoid(x)
def _layernorm(x, axis=-1, eps=1e-5):
    m = x.mean(dim=axis, keepdim=True)
    v = x.var(dim=axis, keepdim=True, unbiased=False)
    return (x - m) / torch.sqrt(v + eps)
def _scatter_add(tgt, idx, src, axis=0):
    return tgt.clone().index_add_(axis, idx, src)
def _segment_sum(src, seg, n):
    out = torch.zeros((n,) + src.shape[1:], dtype=src.dtype)
    return out.index_add_(0, seg, src)
def _pad(x, pw):
    flat = tuple(v for pair in reversed(pw) for v in pair)
    return torch.nn.functional.pad(x, flat)

kernel_name = 'concat_heads__gqa_block_cached__mha_block_cached__near_ridge'

def setup_inputs():

    _w = (lambda a: (lambda t: t.to(torch.bfloat16) if t.is_floating_point() else t)(torch.as_tensor(a)))
    def _A_setup():
        B, Sq, D = 16, 32, 512
        Hq, Hkv, Sk, Dh = 32, 8, 512, 128
        assert Hq % Hkv == 0, (Hq, Hkv)
        return {
            "x": _w((lambda *s: torch.randn(*s, dtype=torch.bfloat16) * 0.8)(B, Sq, D)),
            "k_cache": _w((lambda *s: torch.randn(*s, dtype=torch.bfloat16) * 0.8)(B, Hkv, Sk, Dh)),
            "v_cache": _w((lambda *s: torch.randn(*s, dtype=torch.bfloat16) * 0.05)(B, Hkv, Sk, Dh)),
            "wq": _w((lambda *s: torch.randn(*s, dtype=torch.bfloat16) * 0.8)(D, Hq * Dh) * (D ** -0.5)),
            "wo": _w((lambda *s: torch.randn(*s, dtype=torch.bfloat16) * 0.05)(Hq * Dh, D) * ((Hq * Dh) ** -0.5)),
        }
    _A_in = _A_setup()

    def _B_setup():
        B, Sq, D = 16, 32, 512
        H, Sk, Dh = 32, 512, 128
        return {
            "x": _w((lambda *s: torch.randn(*s, dtype=torch.bfloat16) * 0.8)(B, Sq, D)),
            "k_cache": _w((lambda *s: torch.randn(*s, dtype=torch.bfloat16) * 0.8)(B, H, Sk, Dh)),
            "v_cache": _w((lambda *s: torch.randn(*s, dtype=torch.bfloat16) * 0.05)(B, H, Sk, Dh)),
            "wq": _w((lambda *s: torch.randn(*s, dtype=torch.bfloat16) * 0.8)(D, H * Dh) * (D ** -0.5)),
            "wo": _w((lambda *s: torch.randn(*s, dtype=torch.bfloat16) * 0.05)(H * Dh, D) * ((H * Dh) ** -0.5)),
        }
    _B_in = _B_setup()

    return {
        "A_x": _A_in["x"],
        "A_k_cache": _A_in["k_cache"],
        "A_v_cache": _A_in["v_cache"],
        "A_wq": _A_in["wq"],
        "A_wo": _A_in["wo"],
        "B_x": _B_in["x"],
        "B_k_cache": _B_in["k_cache"],
        "B_v_cache": _B_in["v_cache"],
        "B_wq": _B_in["wq"],
        "B_wo": _B_in["wo"],
    }

def reference(A_x, A_k_cache, A_v_cache, A_wq, A_wo, B_x, B_k_cache, B_v_cache, B_wq, B_wo):
    def _A_ref(x, k_cache, v_cache, wq, wo):
        B, Sq, D = x.shape
        Hq, Dh = 32, 128
        Hkv = k_cache.shape[1]
        rep = Hq // Hkv
        q = (lambda x, *d: x.permute(*d))((x @ wq).reshape(B, Sq, Hq, Dh), 0, 2, 1, 3)
        k = (lambda x, n, axis: torch.repeat_interleave(x, n, dim=axis))(k_cache, rep, axis=1)
        v = (lambda x, n, axis: torch.repeat_interleave(x, n, dim=axis))(v_cache, rep, axis=1)
        scores = torch.einsum("bhqd,bhkd->bhqk", q, k) * (Dh ** -0.5)
        p = _softmax(scores, axis=-1)
        o = torch.einsum("bhqk,bhkd->bhqd", p, v)
        return (lambda x, *d: x.permute(*d))(o, 0, 2, 1, 3).reshape(B, Sq, Hq * Dh) @ wo

    def _B_ref(x, k_cache, v_cache, wq, wo):
        B, Sq, D = x.shape
        H, Dh = 32, 128
        q = (lambda x, *d: x.permute(*d))((x @ wq).reshape(B, Sq, H, Dh), 0, 2, 1, 3)
        scores = torch.einsum("bhqd,bhkd->bhqk", q, k_cache) * (Dh ** -0.5)
        p = _softmax(scores, axis=-1)
        o = torch.einsum("bhqk,bhkd->bhqd", p, v_cache)
        return (lambda x, *d: x.permute(*d))(o, 0, 2, 1, 3).reshape(B, Sq, H * Dh) @ wo

    a = _A_ref(A_x, A_k_cache, A_v_cache, A_wq, A_wo)
    b = _B_ref(B_x, B_k_cache, B_v_cache, B_wq, B_wo)
    return torch.concatenate([a, b], axis=-1)
```

```cpp
#include <hip/hip_runtime.h>
#include <hip/hip_bf16.h>

typedef unsigned v4u __attribute__((ext_vector_type(4)));
typedef unsigned v2u __attribute__((ext_vector_type(2)));
typedef float f32x4 __attribute__((ext_vector_type(4)));
typedef float f32x2 __attribute__((ext_vector_type(2)));
typedef float f32x16 __attribute__((ext_vector_type(16)));
typedef short bf16x8 __attribute__((ext_vector_type(8)));
typedef short s16x4 __attribute__((ext_vector_type(4)));
typedef __bf16 bf2_t __attribute__((ext_vector_type(2)));
typedef unsigned long long u64;
#define DI __device__ __forceinline__
#define LAS __attribute__((address_space(3)))
#define GAS __attribute__((address_space(1)))
#define SBAR() __builtin_amdgcn_sched_barrier(0)
#define CFENCE() asm volatile("" ::: "memory")
#define RLX_AGENT __ATOMIC_RELAXED, __HIP_MEMORY_SCOPE_AGENT


DI f32x16 mfma32(v4u a, v4u b, f32x16 c) {
  return __builtin_amdgcn_mfma_f32_32x32x16_bf16(__builtin_bit_cast(bf16x8, a), __builtin_bit_cast(bf16x8, b), c, 0, 0, 0);
}
DI f32x4 mfma16(v4u a, v4u b, f32x4 c) { return __builtin_amdgcn_mfma_f32_16x16x32_bf16(__builtin_bit_cast(bf16x8, a), __builtin_bit_cast(bf16x8, b), c, 0, 0, 0); }
DI unsigned pk(float lo, float hi) { f32x2 v = {lo, hi}; return __builtin_bit_cast(unsigned, __builtin_convertvector(v, bf2_t)); }
DI v4u lds_b128(unsigned a) { return *(const LAS v4u*)(size_t)(a); }
DI v4u lds_tr2(unsigned a0, unsigned a1) {
  const s16x4 lo = __builtin_amdgcn_ds_read_tr16_b64_v4i16((LAS s16x4*)(size_t)a0);
  const s16x4 hi = __builtin_amdgcn_ds_read_tr16_b64_v4i16((LAS s16x4*)(size_t)a1);
  return __builtin_bit_cast(v4u, __builtin_shufflevector(lo, hi, 0, 1, 2, 3, 4, 5, 6, 7));
}
DI v4u pack8(const f32x16& x, int s) {
  return v4u{pk(x[8 * s], x[8 * s + 1]), pk(x[8 * s + 2], x[8 * s + 3]), pk(x[8 * s + 4], x[8 * s + 5]), pk(x[8 * s + 6], x[8 * s + 7])};
}
DI float max3f(float a, float b, float c) { return __builtin_fmaxf(__builtin_fmaxf(a, b), c); }
DI float xh_max(float x) {
  auto t = __builtin_amdgcn_permlane32_swap(__float_as_uint(x), __float_as_uint(x), false, false);
  return fmaxf(__uint_as_float(t[0]), __uint_as_float(t[1]));
}
#define DMA4(AUX, m0v, desc, soff, v0, v1, v2, v3)                                                             \
  asm volatile("s_mov_b32 m0, %0\n\ts_nop 0\n\t"                                                               \
               "buffer_load_dwordx4 %3, %1, %2 offen" AUX " lds\n\t"                                           \
               "buffer_load_dwordx4 %4, %1, %2 offen offset:1024" AUX " lds\n\t"                               \
               "buffer_load_dwordx4 %5, %1, %2 offen offset:2048" AUX " lds\n\t"                               \
               "buffer_load_dwordx4 %6, %1, %2 offen offset:3072" AUX " lds"                                   \
               :: "s"(m0v), "s"(desc), "s"(soff), "v"(v0), "v"(v1), "v"(v2), "v"(v3) : "memory")

constexpr float SL2E = 0.08838834764831845f * 1.4426950408889634f;
constexpr int NS = 9, SLOT = 16384, CTRL = NS * SLOT;
constexpr int TAB = CTRL + 64;
constexpr int SCR = TAB + 2 * 3 * 256;
constexpr int LDS1 = SCR + 256;
constexpr unsigned SPIN_LDS = 1u << 18, SPIN_GLB = 1u << 13;
constexpr unsigned OF_OFF = 0, FL_OFF = 8u << 20;
constexpr int AUX_SC1 = 16;
constexpr int CODEPF = 25984;
constexpr int BIAS = 4096;

struct Sched { unsigned ld[3][128]; unsigned cs[3][4][64]; unsigned pk[3][2][4]; int n[3]; int nd[3]; };
struct TileT { int src, p1, p2, mask, step, park, rstep; };
constexpr int kT[7] = {3, 2, 8, 20, 4, 2, 5};
constexpr int YRR = kT[0];
constexpr int YL1 = kT[1], YL2 = kT[2], YL3 = kT[3];
constexpr int XPARK = 3;
constexpr int XFIRST = kT[4], XPRE = kT[5], XAHEAD = kT[6];
constexpr int at_isv(int k) { return k == 15 ? 1 : (k >= 2 && !(k & 1)); }
constexpr int at_idx(int k) { return k == 15 ? 7 : k == 0 ? 0 : (k & 1) ? (k + 1) / 2 : k / 2 - 1; }
constexpr Sched make_sched() {
  Sched S{};
  for (int ty = 0; ty < 3; ty++) {
    TileT T[128] = {};
    int n = 0;
    if (ty == 0) {
      for (int hh = 0; hh < 2; hh++) {
        const int ord[2][8] = {{0, 4, 1, 5, 2, 3, 6, 7}, {0, 1, 2, 3, 4, 5, 6, 7}};
        for (int k = 0; k < 8; k++) {
          const int o = ord[hh][k];
          if (o < 4) T[n++] = TileT{0, hh, o, 1 << o, 10 + 5 * hh, 0};
          else T[n++] = TileT{1, 4 * hh + o - 4, 0, 15, 10 + 5 * hh + o - 3, 0};
        }
      }
      int nx[4] = {};
      const int lead[4] = {0, YL1, YL2, YL3};
      for (int k = 0; k < YRR; k++) for (int r = 0; r < 4; r++) { T[n++] = TileT{2 + at_isv(k), at_idx(k), r, 1 << r, 20 + k, 0}; nx[r] = k + 1; }
      while (n < 80) {
        int best = -1;
        for (int r = 0; r < 4; r++) if (nx[r] < 16 && (best < 0 || nx[r] + lead[r] < nx[best] + lead[best])) best = r;
        T[n++] = TileT{2 + at_isv(nx[best]), at_idx(nx[best]), best, 1 << best, 20 + nx[best], 0};
        nx[best]++;
      }
    } else {
      for (int hh = 0; hh < 2; hh++) {
        T[n++] = TileT{4, hh, 0, 15, 10 + 5 * hh, 0};
        for (int j = 0; j < 4; j++) for (int a = 0; a < 4; a++) {
          if (n == XFIRST) for (int g = 0; g < XPRE; g++) T[n++] = TileT{6 + at_isv(g), at_idx(g), 0, 15, 20 + g, 0};
          T[n++] = TileT{5, 4 * hh + j, a, 1 << a, 10 + 5 * hh + 1 + j, 0};
        }
      }
      for (int g = XPRE + 2 * XPARK; g < XPRE + 2 * XPARK + XAHEAD; g++) T[n++] = TileT{6 + at_isv(g), at_idx(g), 0, 15, 20 + g, 0};
      for (int g = XPRE; g < XPRE + 2 * XPARK; g++) T[n++] = TileT{6 + at_isv(g), at_idx(g), 0, 15, 20 + g, 1 + ((g - XPRE) & 1)};
      for (int g = XPRE + 2 * XPARK + XAHEAD; g < 16; g++) T[n++] = TileT{6 + at_isv(g), at_idx(g), 0, 15, 20 + g, 0};
      if (ty == 1) { for (int p = 0; p < 16; p++) for (int w = 0; w < 4; w++) T[n++] = TileT{12 + (p >> 3), p & 7, w, 1 << w, 41 + p + (p >> 2), 0}; }
      else for (int p = 0; p < 8; p++) for (int w = 0; w < 4; w++) T[n++] = TileT{8, p, w, 1 << w, 36 + p, 0};
    }
    S.n[ty] = n;
    int idx[4][64] = {};
    for (int i = 0; i < n; i++) for (int w = 0; w < 4; w++) if (T[i].mask >> w & 1) idx[w][T[i].step] = i;
    int dep[128] = {}, rel[128] = {};
    for (int w = 0; w < 4; w++) {
      int d = 0, t = 0;
      for (int st = 10; st < (ty == 1 ? 60 : ty ? 44 : 36); st++) {
        if (ty == 1 && st >= 36 && (st <= 40 || st % 5 == 0)) continue;
        const int i = idx[w][st];
        d = i > d ? i : d;
        t = (i + 3 > t ? i + 3 : t) + 1;
        const int dn = (st >= 21 && st < 35 && idx[w][st + 1] > d) ? idx[w][st + 1] : d;
        if (dn > dep[i]) dep[i] = dn;
        if (t > rel[i]) rel[i] = t;
      }
    }
    if (ty == 1) for (int st = 34; st < 36; st++) {
      const int i = idx[0][st];
      dep[i] = idx[3][44]; rel[i] = rel[idx[3][44]] + 1; T[i].rstep = 45;
    }
    int nd = 0, np[2] = {};
    int occ[NS] = {};
    for (int s = 0; s < NS; s++) occ[s] = -1;
    for (int i = 0; i < n; i++) {
      int best = -1;
      for (int s = 0; s < NS; s++) {
        if (occ[s] >= 0 && (dep[occ[s]] >= i || (ty == 2 && T[occ[s]].step == 43))) continue;
        if (best < 0 || (occ[best] >= 0 && (occ[s] < 0 || rel[occ[s]] < rel[occ[best]]))) best = s;
      }
      if (best < 0) { S.n[ty] = -1; return S; }
      const int pm = occ[best] >= 0 ? T[occ[best]].mask : 0, ps = occ[best] >= 0 ? (T[occ[best]].rstep ? T[occ[best]].rstep : T[occ[best]].step) : 0;
      const unsigned ent = (unsigned)(T[i].src | T[i].p1 << 4 | T[i].p2 << 7 | best << 9 | pm << 13 | ps << 17);
      int owner = 0, oi = 0;
      if (T[i].park) { owner = 1 + T[i].park; oi = np[T[i].park - 1]++; S.pk[ty][T[i].park - 1][oi] = ent; }
      else { owner = nd & 1; oi = nd >> 1; S.ld[ty][nd++] = ent; }
      for (int w = 0; w < 4; w++) if (T[i].mask >> w & 1) S.cs[ty][w][T[i].step] = (unsigned)(oi | best << 8 | owner << 12);
      occ[best] = i;
    }
    S.nd[ty] = nd;
  }
  return S;
}
constexpr Sched kSchedH = make_sched();
static_assert(kSchedH.n[0] == 80 && kSchedH.n[1] == 114 && kSchedH.n[2] == 82 && kSchedH.ld[1][3] == kSchedH.ld[2][3] && kSchedH.pk[1][1][2] == kSchedH.pk[2][1][2] && kSchedH.pk[1][0][2] == kSchedH.pk[2][0][2], "schedule");
__constant__ Sched kSched = make_sched();

DI u64 sel64(bool c, u64 a, u64 b) { const u64 m = c ? ~0ull : 0ull; return (a & m) | (b & ~m); }
DI int op_head(int p1, int w, int b0, int nt) { return 4 * ((2 * w + (p1 & 1) + 2 * nt) & 7) + ((b0 - (p1 >> 1)) & 3); }
DI u64 tile_addr(unsigned e, u64 pxA, u64 pxB, u64 pwA, u64 pwB, u64 pkA, u64 pkB, u64 pvA, u64 pvB, u64 pwo, u64 pwa, int rot, int i16, int b0, int nt) {
  const int p1 = (e >> 4) & 7, p2 = (e >> 7) & 3;
  const bool isA = e & 4, odd = e & 1, kv = e & 2, wo = e & 8;
  const int hr = (i16 >> 2) & 2;
  const int pb = (p2 + i16) & 3;
  const u64 bA = sel64(kv, sel64(odd, pvA, pkA), sel64(odd, pwA, pxA)), bB = sel64(kv, sel64(odd, pvB, pkB), sel64(odd, pwB, pxB));
  const unsigned ox = (isA ? 0 : pb * 32768) + ((p1 ^ (rot >> 2)) & 1) * 512;
  const unsigned ow = (((p1 ^ rot) & 4) | ((p1 + rot) & 3)) * 524288 + (isA ? ((p2 + hr) & 3) * 256 : 0);
  const unsigned ok = (isA ? 0 : pb * (32 * 131072)) + p1 * 16384;
  const int jj = ((p1 ^ rot) & 4) | ((p1 + rot) & 3);
  const unsigned oo = isA ? ((((jj >> 1) + hr) & 3) * 2 + (jj & 1)) * 65536 + p2 * 256 + (odd ? 524288 : 0) : op_head(p1, p2, b0, nt) * 131072;
  return sel64(wo, sel64(isA, pwa, pwo), sel64(isA, bA, bB)) + (wo ? oo : kv ? ok : odd ? ow : ox) - BIAS;
}
DI u64 mix64(u64 z) { z += 0x9E3779B97F4A7C15ull; z = (z ^ (z >> 30)) * 0xBF58476D1CE4E5B9ull; z = (z ^ (z >> 27)) * 0x94D049BB133111EBull; return z ^ (z >> 31); }

__global__ __launch_bounds__(512, 2) void k_fused(const void* xA, const void* kA, const void* vA, const void* wqA, const void* woA,
                                                  const void* xB, const void* kB, const void* vB, const void* wqB, const void* woB, void* out, void* ws) {
  extern __shared__ __attribute__((aligned(16))) char smem[];
  {
    u64 pc; unsigned o, o2;
    asm volatile("s_getpc_b64 %0\n\tv_lshlrev_b32 %1, 7, %3\n\ts_mov_b32 m0, %4\n\tv_min_u32 %2, %6, %1\n\tv_min_u32 %1, %5, %1\n\t"
                 "global_load_lds_dword %1, %0\n\tglobal_load_lds_dword %2, %7"
                 : "=&s"(pc), "=&v"(o), "=&v"(o2) : "v"(threadIdx.x), "i"(SCR), "i"(CODEPF), "i"((int)sizeof(Sched) - 128), "s"(&kSched) : "memory");
    const unsigned t = threadIdx.x, b = blockIdx.x;
    const unsigned off = (b & 8) ? ((b >> 4) * 32 + (t >> 3)) * 8192 + (b & 7) * 1024 + (t & 7) * 128 : (b >> 4) * 32768 + t * 128;
    if (__builtin_amdgcn_readfirstlane(t) < 256)
      asm volatile("global_load_lds_dword %0, %1" :: "v"(off), "s"((b & 8) ? wqB : xB) : "memory");
  }
  const int lane = threadIdx.x & 63, w = __builtin_amdgcn_readfirstlane(threadIdx.x >> 6);
  const int blk = blockIdx.x, xcd = blk & 7, idx = blk >> 3, isX = idx & 1, i16 = idx >> 1;
  const int hq = xcd * 4 + (i16 & 3), bg = (i16 >> 2) * 4;
  const int rot = (i16 * isX + (2 * (i16 >> 2) + 4 * i16) * (1 - isX)) & 7;
  const int obr = xcd & 1, ob0 = 8 * ((xcd >> 1) & 1) + (i16 >> 2), ont = 4 * (xcd >> 2) + (i16 & 3);
  const int tyi = isX + (isX & obr);
  volatile LAS unsigned* ctrl = (volatile LAS unsigned*)(unsigned)CTRL;
  GAS u64* flags = (GAS u64*)((char*)ws + FL_OFF);
  if (__builtin_expect(w >= 4, 1)) {
    const u64 pxA = (u64)xA + i16 * 32768, pxB = (u64)xB + bg * 32768, pwA = (u64)wqA + xcd * 1024, pwB = (u64)wqB + hq * 256;
    const u64 pkA = (u64)kA + (i16 * 8 + xcd) * 131072, pvA = (u64)vA + (i16 * 8 + xcd) * 131072;
    const u64 pkB = (u64)kB + (bg * 32 + hq) * 131072, pvB = (u64)vB + (bg * 32 + hq) * 131072;
    const u64 pwo = (u64)woB + ont * 128, pwa = (u64)woA + xcd * 524288;
#define TILE_ADDR(e) tile_addr(e, pxA, pxB, pwA, pwB, pkA, pkB, pvA, pvB, pwo, pwa, rot, i16, ob0, ont)
    const int rw = lane >> 4;
    if (__builtin_expect(w <= 5, 0)) {
      u64 tag = 0;
      const int P = w - 4;
      unsigned pe[XPARK + 1];
      v4u R[XPARK + 1][16];
      v2u te = {0u, 0u};
      if (w == 5) te = *(const v2u*)&kSched.ld[tyi][2 * lane]; else ctrl[13] = 0u;
      SBAR();
#define PARK_LOAD(t) do { constexpr unsigned c0_ = kSchedH.pk[1][0][t], c1_ = kSchedH.pk[1][1][t];                           \
        static_assert((c0_ & 14) == 6 && (c1_ & 14) == 6, "parked tiles: kA / vA");                                           \
        pe[t] = P ? c1_ : c0_;                                                                                               \
        const GAS char* src_ = (const GAS char*)((pe[t] & 1) ? pvA : pkA) + ((pe[t] >> 4) & 7) * 16384 + ((lane >> 2) & 7) * 256 + (lane >> 5) * 64; \
        _Pragma("unroll") for (int i = 0; i < 16; i++)                                                                       \
          R[t][i] = __builtin_nontemporal_load((const GAS v4u*)(src_ + (i >> 1) * 2048 + (i & 1) * 128 + ((lane & 3) ^ (((i & 2) | (rw & 1)))) * 16)); } while (0)
      if (w == 5) {
        LAS unsigned* tab = (LAS unsigned*)(unsigned)TAB;
        if (lane < 13 && (lane & 14) != 4) ctrl[lane] = 0u;
        const u64 a0 = TILE_ADDR(te[0]), a1 = TILE_ADDR(te[1]);
        tab[lane] = te[0]; tab[64 + lane] = (unsigned)a0; tab[128 + lane] = (unsigned)(a0 >> 32);
        tab[192 + lane] = te[1]; tab[256 + lane] = (unsigned)a1; tab[320 + lane] = (unsigned)(a1 >> 32);
      }
      asm volatile("s_waitcnt lgkmcnt(0)" ::: "memory");
      if (!isX) __builtin_amdgcn_s_barrier();
#define GET_TAG() do { tag = mix64(__hip_atomic_load(flags + 1536, RLX_AGENT));                                              \
        ctrl[4] = (unsigned)tag; ctrl[5] = (unsigned)(tag >> 32); ctrl[13] = 1u;                                             \
        if (lane == 0) __hip_atomic_store(flags + 1024 + blk, tag, RLX_AGENT);     } while (0)
      if (!isX) {
        if (w == 4) {
          unsigned sp = 0;
#pragma unroll 1
          while ((int)ctrl[8] < 20) { if (++sp > SPIN_LDS) break; __builtin_amdgcn_s_sleep(16); }
          GET_TAG();
        }
        return;
      }
      if (w == 4) __builtin_amdgcn_s_sleep(20);
      static_assert(XPARK == 3, "");
      PARK_LOAD(0); PARK_LOAD(1); PARK_LOAD(2);
      SBAR();
      __builtin_amdgcn_s_barrier();
      SBAR();
      if (w == 4) GET_TAG();
#pragma unroll
      for (int t = 0; t < XPARK; t++) {
        const unsigned pm = (pe[t] >> 13) & 15, slot = ((pe[t] >> 9) & 15) * SLOT;
        const int ps = pe[t] >> 17;
        unsigned sp = 0;
#pragma unroll 1
        for (;;) {
          const int prog = (int)ctrl[8 + (lane & 7)];
          if (((unsigned)__ballot(prog <= ps) & pm) == 0u || ++sp > SPIN_LDS) break;
          __builtin_amdgcn_s_sleep(2);
        }
        CFENCE();
#pragma unroll
        for (int i = 0; i < 16; i++) *(LAS v4u*)(size_t)(slot + i * 1024 + lane * 16) = R[t][i];
        CFENCE();
        ctrl[2 + P] = (unsigned)(t + 1);
        CFENCE();
      }
      if (isX) {
        if (w == 4 && obr) {
          const GAS u64* fl = flags + (16 + ob0 + 4 * (lane >> 5)) * 32 + (lane & 31);
          unsigned sp = 0;
#pragma unroll 1
          while ((int)ctrl[8] < 35) { if (++sp > SPIN_LDS) break; __builtin_amdgcn_s_sleep(16); }
          sp = 0;
#pragma unroll 1
          for (;;) {
            const u64 got = __ballot(__hip_atomic_load(fl, RLX_AGENT) == tag);
            const unsigned both = (unsigned)got & (unsigned)(got >> 32);
            ctrl[6] = both;
            if (both == 0xffffffffu || ++sp > SPIN_GLB) break;
            __builtin_amdgcn_s_sleep(4);
          }
        }
        if (w == 4 && blk == 8) {
          unsigned sp = 0;
#pragma unroll 1
          for (;;) {
            bool ok = true;
#pragma unroll
            for (int i = 0; i < 4; i++) ok &= __hip_atomic_load(flags + 1024 + i * 64 + lane, RLX_AGENT) == tag;
            if (__all(ok) || ++sp > SPIN_GLB) break;
            __builtin_amdgcn_s_sleep(64);
          }
          if (lane == 0) __hip_atomic_store(flags + 1536, tag, RLX_AGENT);
        }
      }
      return;
    }
    __builtin_amdgcn_s_setprio(3);
    const int L = w - 6;
    unsigned e0;
    {
      const unsigned ea = L ? (isX ? kSchedH.ld[1][1] : kSchedH.ld[0][1]) : (isX ? kSchedH.ld[1][0] : kSchedH.ld[0][0]);
      const unsigned eb = L ? (isX ? kSchedH.ld[1][3] : kSchedH.ld[0][3]) : (isX ? kSchedH.ld[1][2] : kSchedH.ld[0][2]);
      e0 = lane ? eb : ea;
    }
    const u64 a0 = TILE_ADDR(e0);
    unsigned lo = (unsigned)a0, hi = (unsigned)(a0 >> 32);
    int vk[4], vw[4], vx[4], vo[4];
#pragma unroll
    for (int i = 0; i < 4; i++) {
      const int row = 8 * (i >> 1) + ((lane >> 2) & 7), c = (8 * (i & 1) + 4 * (lane >> 5) + ((lane & 3) ^ ((row >> 2) & 3))) * 16 + BIAS - 1024 * i;
      vk[i] = row * 256 + c;
      vw[i] = row * 8192 + c;
      vx[i] = row * 1024 + c;
      vo[i] = (8 * i + (lane >> 3)) * 1024 + 16 * ((lane & 7) ^ ((rw & 1) << 2)) + BIAS - 1024 * i;
    }
    const int cnt = ((isX ? (obr ? kSchedH.nd[2] : kSchedH.nd[1]) : kSchedH.nd[0]) + 1 - L) >> 1;
    const unsigned fullw = CTRL + 4 * L, progw = CTRL + 32 + (lane & 7) * 4;
    int prog = 0;
    unsigned e = __builtin_amdgcn_readlane(e0, 0);
    v4u desc = {(unsigned)__builtin_amdgcn_readlane(lo, 0), (unsigned)__builtin_amdgcn_readlane(hi, 0), 0x81000u, 0x00020000u};
    asm volatile("s_nop 4");
#pragma unroll 1
    for (int i = 0; i < cnt; i++) {
      const unsigned pm = (e >> 13) & 15, slot = ((e >> 9) & 15) * SLOT;
      const int ps = e >> 17;
      if (__builtin_expect(((unsigned)__ballot(prog <= ps) & pm) != 0u, 0)) {
        unsigned sp = 0;
#pragma unroll 1
        for (;;) {
          asm volatile("ds_read_b32 %0, %1\n\ts_waitcnt lgkmcnt(0)" : "=&v"(prog) : "v"(progw) : "memory");
          if (((unsigned)__ballot(prog <= ps) & pm) == 0u || ++sp > SPIN_LDS) break;
          asm volatile("s_waitcnt vmcnt(0)\n\tds_write_b32 %0, %1" :: "v"(fullw), "v"(i) : "memory");
          __builtin_amdgcn_s_sleep(1);
        }
      }
      unsigned en; v4u dn;
#define NEXT_DESC() do { en = __builtin_amdgcn_readlane(e0, i + 1); dn = v4u{(unsigned)__builtin_amdgcn_readlane(lo, i + 1), (unsigned)__builtin_amdgcn_readlane(hi, i + 1), 0x81000u, 0x00020000u}; \
                         asm volatile("" : "+s"(en), "+s"(dn)); } while (0)
      if (e & 2) {
        DMA4(" nt", slot, desc, 0, vk[0], vk[1], vk[2], vk[3]);
        DMA4(" nt", slot + 4096, desc, 4096, vk[0], vk[1], vk[2], vk[3]);
        NEXT_DESC();
        DMA4(" nt", slot + 8192, desc, 8192, vk[0], vk[1], vk[2], vk[3]);
        asm volatile("ds_read_b32 %0, %1" : "=v"(prog) : "v"(progw) : "memory");
        DMA4(" nt", slot + 12288, desc, 12288, vk[0], vk[1], vk[2], vk[3]);
      } else {
        const bool isw = (e & 9) == 1, iso = (e & 12) == 8;
        const int gs = iso ? 32768 : isw ? 131072 : 16384, g2 = (e & 9) ? 2 * gs : 256;
        const int u0 = iso ? vo[0] : isw ? vw[0] : vx[0], u1 = iso ? vo[1] : isw ? vw[1] : vx[1], u2 = iso ? vo[2] : isw ? vw[2] : vx[2], u3 = iso ? vo[3] : isw ? vw[3] : vx[3];
        DMA4("", slot, desc, 0, u0, u1, u2, u3);
        DMA4("", slot + 4096, desc, gs, u0, u1, u2, u3);
        NEXT_DESC();
        DMA4("", slot + 8192, desc, g2, u0, u1, u2, u3);
        asm volatile("ds_read_b32 %0, %1" : "=v"(prog) : "v"(progw) : "memory");
        DMA4("", slot + 12288, desc, g2 + gs, u0, u1, u2, u3);
      }
      if (__builtin_expect(i == 1, 0)) {
        __builtin_amdgcn_s_barrier();
        asm volatile("ds_read_b32 %0, %3\n\tds_read_b32 %1, %3 offset:256\n\tds_read_b32 %2, %3 offset:512\n\ts_waitcnt lgkmcnt(0)"
                     : "=&v"(e0), "=&v"(lo), "=&v"(hi) : "v"(TAB + L * 768 + lane * 4) : "memory");
        NEXT_DESC();
        prog = 0;
      }
      asm volatile("s_waitcnt vmcnt(16) lgkmcnt(0)\n\tds_write_b32 %1, %2" : "+v"(prog) : "v"(fullw), "v"(i) : "memory");
      e = en; desc = dn;
    }
    asm volatile("s_waitcnt vmcnt(0)\n\tds_write_b32 %0, %1" :: "v"(fullw), "v"(cnt) : "memory");
    return;
  }
  const unsigned cst = kSched.cs[tyi][w][lane];
  const __amdgpu_buffer_rsrc_t rws = __builtin_amdgcn_make_buffer_rsrc(ws, 0, (int)FL_OFF, 0x00020000);
  const int bw = bg + ((w + i16) & 3);
  const int unit = isX ? i16 * 32 + xcd * 4 + ((w + ((i16 >> 2) & 2)) & 3) : (16 + bw) * 32 + hq;
  __builtin_amdgcn_s_barrier();
  CFENCE();
  u64 tag = 0;
  int pf = 0;
  auto ready = [&](unsigned e) -> bool { return __builtin_amdgcn_readlane(pf, e >> 12) > (int)(e & 255); };
  auto wait_for = [&](unsigned e) {
    const int t = e & 255;
    unsigned sp = 0;
#pragma unroll 1
    for (;;) {
      const int f = __builtin_amdgcn_readfirstlane(ctrl[e >> 12]);
      if (f > t || ++sp > SPIN_LDS) break;
    }
  };
  auto acquire = [&](int st) -> unsigned {
    const unsigned e = __builtin_amdgcn_readlane(cst, st);
    if (__builtin_expect(!ready(e), 0)) wait_for(e);
    CFENCE();
    return ((e >> 8) & 15) * SLOT;
  };
  auto release = [&](int st) { CFENCE(); ctrl[8 + w] = (unsigned)(st + 1); CFENCE(); };
  auto repoll = [&]() { pf = (int)ctrl[lane & 7]; };
  auto publish = [&](const v4u* fr, unsigned off, int flag) {
#pragma unroll
    for (int f = 0; f < 8; f++) __builtin_amdgcn_raw_buffer_store_b128(fr[f], rws, lane * 16 + f * 1024, off, AUX_SC1);
    asm volatile("s_waitcnt vmcnt(0)" ::: "memory");
    if (lane == 0) __hip_atomic_store(flags + flag, tag, RLX_AGENT);
  };
  const int r32 = lane & 31, h = lane >> 5, blkk = (lane >> 4) & 1, q4 = (lane & 15) >> 2, p4 = lane & 3;
  const unsigned rowb = 2048 * (r32 >> 3) + 64 * (r32 & 7) + 16 * (h ^ ((r32 >> 2) & 3));
  const unsigned trv = 64 * (4 * h + q4) + 16 * ((2 * blkk + (p4 >> 1)) ^ h) + 8 * (p4 & 1);
  f32x16 acc[4];
  v4u F[16], Q[8];
#define ZERO_ACC() do { _Pragma("unroll") for (int c_ = 0; c_ < 4; c_++) _Pragma("unroll") for (int e_ = 0; e_ < 16; e_++) acc[c_][e_] = 0.f; } while (0)
#define ROWRD(a0, a1, s_, off) lds_b128((((s_) & 1) ? (a1) : (a0)) + 512 * ((s_) >> 1) + (off))
#define TRRD(a0, a1, i_, off) lds_tr2((a0) + ((i_) >> 2) * 4096 + ((i_) & 3) * 512 + (off), (a1) + ((i_) >> 2) * 4096 + ((i_) & 3) * 512 + (off))
#define PROJ(HF0, NH, XSLOT, TRB, TX, TA) do {                                                                \
    ZERO_ACC();                                                                                               \
    _Pragma("unroll 1") for (int hf = HF0; hf < HF0 + NH; hf++) {                                              \
      v4u X[16];                                                                                              \
      {                                                                                                       \
        if (NH == 2 || hf < HF0 + 2) {                                                                        \
          const unsigned sb = (XSLOT) + rowb;                                                                 \
          _Pragma("unroll") for (int j = 0; j < 4; j++) {                                                     \
            const unsigned a = sb + ((j + rot) & 1) * 1024 + ((j + rot) & 2) * 4096;                          \
            _Pragma("unroll") for (int ks = 0; ks < 4; ks++) X[4 * j + ks] = ROWRD(a, a ^ 32, ks, 0);         \
          }                                                                                                   \
        } else {                                    \
          unsigned sp = 0;                                                                                    \
          _Pragma("unroll 1") while (!__all(__hip_atomic_load(flags + i16 * 32 + xcd * 4 + 4 + (lane & 3), RLX_AGENT) == tag)) { if (++sp > SPIN_GLB) break; __builtin_amdgcn_s_sleep(8); } \
          __builtin_amdgcn_fence(__ATOMIC_ACQUIRE, "wavefront");                                              \
          _Pragma("unroll") for (int j = 0; j < 4; j++) {                                                     \
            const int jj = (((hf << 2) ^ rot) & 4) | ((j + rot) & 3);                                         \
            const unsigned go = OF_OFF + (i16 * 32 + xcd * 4 + 4 + (((jj >> 1) + ((i16 >> 2) & 2)) & 3)) * 8192 + (jj & 1) * 4096; \
            _Pragma("unroll") for (int ks = 0; ks < 4; ks++) X[4 * j + ks] = __builtin_amdgcn_raw_buffer_load_b128(rws, lane * 16 + ks * 1024, go, AUX_SC1); \
          }                                                                                                   \
        }                                                                                                     \
        repoll();                                                                                             \
        release(5 * hf);                                                                                      \
      }                                                                                                       \
      unsigned a0 = acquire(5 * hf + 1) + (TRB), a1 = (a0 ^ TX) + TA;                                         \
      _Pragma("unroll") for (int i = 0; i < 12; i++) F[i] = TRRD(a0, a1, i, 0);                               \
      _Pragma("unroll") for (int j = 0; j < 4; j++) {                                                         \
        SBAR();                                                                                               \
        _Pragma("unroll") for (int i = 0; i < 2; i++) { acc[i] = mfma32(F[i], X[4 * j], acc[i]); F[12 + 2 * i] = TRRD(a0, a1, 12 + 2 * i, 0); F[13 + 2 * i] = TRRD(a0, a1, 13 + 2 * i, 0); SBAR(); } \
        release(5 * hf + 1 + j);                                                                              \
        SBAR();                                                                                               \
        _Pragma("unroll") for (int i = 2; i < 12; i++) { acc[i & 3] = mfma32(F[i], X[4 * j + (i >> 2)], acc[i & 3]); if (i == 8) { repoll(); SBAR(); } } \
        SBAR();                                                                                               \
        if (j < 3) { a0 = acquire(5 * hf + 2 + j) + (TRB); a1 = (a0 ^ TX) + TA; }                             \
        SBAR();                                                                                               \
        _Pragma("unroll") for (int i = 0; i < 4; i++) {                                                       \
          acc[i] = mfma32(F[12 + i], X[4 * j + 3], acc[i]);                                                   \
          if (j < 3) { F[3 * i] = TRRD(a0, a1, 3 * i, 0); F[3 * i + 1] = TRRD(a0, a1, 3 * i + 1, 0); F[3 * i + 2] = TRRD(a0, a1, 3 * i + 2, 0); } \
          SBAR();                                                                                             \
        }                                                                                                     \
      }                                                                                                       \
    }                                                                                                         \
  } while (0)
  {
    const unsigned trw = 2048 * h + 64 * q4 + 16 * ((2 * blkk + (p4 & 1)) ^ (2 * h)) + 8 * (p4 >> 1);
    PROJ(2, 2, acquire(5 * hf), trw, 16, 256);
#pragma unroll
    for (int c = 0; c < 4; c++) {
#pragma unroll
      for (int e = 0; e < 16; e++) acc[c][e] *= SL2E;
      Q[2 * c] = pack8(acc[c], 0); Q[2 * c + 1] = pack8(acc[c], 1);
    }
  }
  f32x16 sa, sb_, negm;
  unsigned k0, k1, v0 = trv, v1 = trv;
  {
    k0 = acquire(20) + rowb; k1 = k0 ^ 32;
#pragma unroll
    for (int i = 0; i < 8; i++) F[8 + i] = ROWRD(k0, k1, i, 0);
#pragma unroll
    for (int i = 0; i < 8; i++) F[i] = ROWRD(k0, k1, i, 8192);
    release(20);
    repoll();
#pragma unroll
    for (int e = 0; e < 16; e++) sa[e] = 0.f;
#pragma unroll
    for (int s = 0; s < 8; s++) sa = mfma32(F[8 + s], Q[s], sa);
    float mx = max3f(sa[0], sa[1], sa[2]);
#pragma unroll
    for (int e = 3; e < 15; e += 2) mx = max3f(mx, sa[e], sa[e + 1]);
    mx = xh_max(fmaxf(mx, sa[15]));
#pragma unroll
    for (int e = 0; e < 16; e++) { sa[e] -= mx; negm[e] = -mx; }
  }
  ZERO_ACC();
  float al = 1.f, linv;
  bool pend = false;
  unsigned PA[8], PB[8];
  f32x4 lacc = {0.f, 0.f, 0.f, 0.f};
  const unsigned on_ = ((lane & 15) == ((lane >> 4) & 1)) ? 0x3f803f80u : 0u;
  const v4u ones = {on_, on_, on_, on_};
#pragma unroll
  for (int i = 0; i < 8; i++) PB[i] = 0u;
#define EXPSTEP(SC, PN, e_) do { const float p_ = __builtin_amdgcn_exp2f(SC[e_]); if ((e_) & 1) { PN[(e_) >> 1] = pk(q_, p_); asm volatile("" : "+v"(PN[(e_) >> 1])); } else { q_ = p_; asm volatile("" : "+v"(q_)); } } while (0)
#define RESCALE() do { _Pragma("unroll") for (int c_ = 0; c_ < 4; c_++) _Pragma("unroll") for (int e_ = 0; e_ < 16; e_++) acc[c_][e_] *= al; \
                       lacc[0] *= al; lacc[1] *= __shfl(al, (lane + 16) & 63); } while (0)
#define STAGE(SC, SN, PN, PP, voff, MID, koff, DOPV, DOQK, DOEXP) do {                                       \
    float q_ = 0.f;                                                                                          \
    SBAR();                                                                                                  \
    _Pragma("unroll") for (int i_ = 0; i_ < 8; i_++) {                                                       \
      if (DOQK) SN = mfma32(F[i_], Q[i_], i_ ? SN : negm);                                                   \
      if (DOPV) F[8 + i_] = TRRD(v0, v1, i_, voff);                                                          \
      if (DOEXP) EXPSTEP(SC, PN, i_);                                                                        \
      if (i_ == 3) repoll();                                                                                 \
      SBAR();                                                                                                \
    }                                                                                                        \
    const v4u P0_ = {PP[0], PP[1], PP[2], PP[3]}, P1_ = {PP[4], PP[5], PP[6], PP[7]};                        \
    if (DOPV) { lacc = mfma16(ones, P0_, lacc); lacc = mfma16(ones, P1_, lacc); }                            \
    MID;                                                                                                     \
    SBAR();                                                                                                  \
    float mx_ = 0.f;                                                                                         \
    _Pragma("unroll") for (int i_ = 0; i_ < 8; i_++) {                                                       \
      if (DOPV) acc[i_ & 3] = mfma32(F[8 + i_], i_ < 4 ? P0_ : P1_, acc[i_ & 3]);                            \
      if (DOQK) F[i_] = ROWRD(k0, k1, i_, koff);                                                             \
      if (DOEXP) EXPSTEP(SC, PN, 8 + i_);                                                                    \
      if (DOQK) { mx_ = max3f(mx_, SN[2 * i_], SN[2 * i_ + 1]); asm volatile("" : "+v"(mx_)); }              \
      if (i_ == 3) repoll();                                                                                 \
      SBAR();                                                                                                \
    }                                                                                                        \
      \
    if (__builtin_expect(pend | __any(mx_ > 8.0f), 0)) {                                                     \
      RESCALE();                                                                                             \
      pend = __any(mx_ > 8.0f);                                                                              \
      const float d_ = pend ? xh_max(mx_) : 0.f;                                                             \
      al = __builtin_amdgcn_exp2f(-d_);                                                                      \
      if (DOQK) { _Pragma("unroll") for (int e_ = 0; e_ < 16; e_++) { SN[e_] -= d_; negm[e_] -= d_; } }      \
    }                                                                                                        \
  } while (0)
#define SETK(st) do { k0 = acquire(st) + rowb; k1 = k0 ^ 32; } while (0)
#define SETV(st) do { v0 = acquire(st) + trv; v1 = (v0 ^ 32) + 2048; } while (0)
  STAGE(sa, sb_, PA, PB, 0, SETK(21), 0, false, true, true);
#pragma unroll 1
  for (int it = 0; it < 7; it++) {
    SETV(22 + 2 * it);
    STAGE(sb_, sa, PB, PA, 0, {}, 8192, true, true, true);
    release(21 + 2 * it);
    STAGE(sa, sb_, PA, PB, 8192, { release(22 + 2 * it); if (it < 6) SETK(23 + 2 * it); }, 0, true, true, true);
  }
  {
    unsigned sp = 0;
#pragma unroll 1
    while (ctrl[13] == 0u) { if (++sp > SPIN_LDS) break; }
    tag = (u64)(unsigned)__builtin_amdgcn_readfirstlane(ctrl[4]) | (u64)(unsigned)__builtin_amdgcn_readfirstlane(ctrl[5]) << 32;
  }
  {
    float q_ = 0.f;
#pragma unroll
    for (int e = 0; e < 16; e++) EXPSTEP(sb_, PB, e);
    const v4u A0 = {PA[0], PA[1], PA[2], PA[3]}, A1 = {PA[4], PA[5], PA[6], PA[7]}, B0_ = {PB[0], PB[1], PB[2], PB[3]}, B1_ = {PB[4], PB[5], PB[6], PB[7]};
    lacc = mfma16(ones, A0, lacc); lacc = mfma16(ones, A1, lacc);
    if (__builtin_expect(pend, 0)) { lacc[0] *= al; lacc[1] *= __shfl(al, (lane + 16) & 63); }
    lacc = mfma16(ones, B0_, lacc); lacc = mfma16(ones, B1_, lacc);
    const float l0_ = __shfl(lacc[0], lane & 15), l1_ = __shfl(lacc[1], lane & 15);
    linv = 1.0f / (r32 < 16 ? l0_ : l1_);
    asm volatile("" : "+v"(linv));
    repoll();
    SETV(35);
#pragma unroll
    for (int i = 0; i < 16; i++) F[i] = TRRD(v0, v1, i & 7, (i >> 3) * 8192);
    release(35);
    SBAR();
#pragma unroll
    for (int i = 0; i < 8; i++) acc[i & 3] = mfma32(F[i], i < 4 ? A0 : A1, acc[i & 3]);
    if (__builtin_expect(pend, 0)) {
#pragma unroll
      for (int c = 0; c < 4; c++)
#pragma unroll
        for (int e = 0; e < 16; e++) acc[c][e] *= al;
    }
#pragma unroll
    for (int i = 0; i < 8; i++) acc[i & 3] = mfma32(F[8 + i], i < 4 ? B0_ : B1_, acc[i & 3]);
  }
  {
    const float inv = linv;
    v4u of[8];
#pragma unroll
    for (int c = 0; c < 4; c++) {
#pragma unroll
      for (int e = 0; e < 16; e++) acc[c][e] *= inv;
      of[2 * c] = pack8(acc[c], 0); of[2 * c + 1] = pack8(acc[c], 1);
    }
    if (!isX || obr) publish(of, OF_OFF + unit * 8192, unit);
    if (!isX) return;
    if (!obr) {
    auto wait_all = [&](int v) {
      unsigned sp = 0;
#pragma unroll 1
      while (((unsigned)__ballot((int)ctrl[8 + (lane & 7)] < v) & 15u) != 0u) { if (++sp > SPIN_LDS) break; }
      CFENCE();
    };
    constexpr unsigned XS0 = ((kSchedH.cs[1][0][34] >> 8) & 15) * SLOT, XS1 = ((kSchedH.cs[1][0][35] >> 8) & 15) * SLOT;
    wait_all(36);
    const unsigned xa = ((w & 2) ? XS1 : XS0) + (w & 1) * 8192 + rowb;
#pragma unroll
    for (int f = 0; f < 8; f++) *(LAS v4u*)(size_t)(((f & 1) ? (xa ^ 32) : xa) + 512 * (f >> 1)) = of[f];
    release(36);
    wait_all(37);
    const unsigned tra = 64 * (4 * h + q4) + 16 * ((2 * blkk + (p4 & 1)) ^ h) + 8 * (p4 >> 1);
    PROJ(8, 4, (((hf ^ (rot >> 2)) & 1) ? XS1 : XS0), tra, 32, 2048);
#pragma unroll
    for (int c = 0; c < 4; c++) { of[2 * c] = pack8(acc[c], 0); of[2 * c + 1] = pack8(acc[c], 1); }
    const int pu = i16 * 32 + xcd * 4 + w;
    publish(of, OF_OFF + pu * 8192, pu);
    }
  }
  const __amdgpu_buffer_rsrc_t rout = __builtin_amdgcn_make_buffer_rsrc(out, 0, 1 << 20, 0x00020000);
  if (!obr) {
    const int p0 = i16 * 32 + (xcd >> 1);
    unsigned sp = 0;
#pragma unroll 1
    while (!__all(__hip_atomic_load(flags + p0 + 8 * (lane & 3), RLX_AGENT) == tag)) { if (++sp > SPIN_GLB) break; __builtin_amdgcn_s_sleep(4); }
    __builtin_amdgcn_fence(__ATOMIC_ACQUIRE, "wavefront");
#pragma unroll
    for (int i = 0; i < 8; i++) F[i] = __builtin_amdgcn_raw_buffer_load_b128(rws, lane * 16 + (i & 1) * 1024, OF_OFF + (p0 + 8 * (i >> 1)) * 8192 + w * 2048, AUX_SC1);
#pragma unroll
    for (int s = 0; s < 2; s++) {
      v4u o;
#pragma unroll
      for (int d = 0; d < 4; d++) {
        float lo = 0.f, hi = 0.f;
#pragma unroll
        for (int g = 0; g < 4; g++) { lo += __uint_as_float(F[2 * g + s][d] << 16); hi += __uint_as_float(F[2 * g + s][d] & 0xffff0000u); }
        o[d] = pk(lo, hi);
      }
      __builtin_amdgcn_raw_buffer_store_b128(o, rout, ((i16 * 32 + r32) * 1024 + (xcd >> 1) * 128 + 32 * w + 16 * s + 8 * h) * 2, 0, 0);
    }
    return;
  }
  {
    const unsigned tro = (4 * h + q4) * 128 + 16 * ((2 * blkk + (p4 & 1)) ^ ((q4 >> 1) << 2)) + 8 * (p4 >> 1);
    unsigned got = 0;
    auto fetch = [&](int p, v4u (&B)[16]) {
      const int hd = op_head(p, w, ob0, ont);
      unsigned sp = 0;
#pragma unroll 1
      while (!((got >> hd) & 1)) {
        got = __builtin_amdgcn_readfirstlane(ctrl[6]);
        if (((got >> hd) & 1) || ++sp > SPIN_LDS) break;
        __builtin_amdgcn_s_sleep(2);
      }
      __builtin_amdgcn_fence(__ATOMIC_ACQUIRE, "wavefront");
      const unsigned off = OF_OFF + ((16 + ob0) * 32 + hd) * 8192;
#pragma unroll
      for (int f = 0; f < 8; f++) {
        B[2 * f] = __builtin_amdgcn_raw_buffer_load_b128(rws, lane * 16 + f * 1024, off, AUX_SC1);
        B[2 * f + 1] = __builtin_amdgcn_raw_buffer_load_b128(rws, lane * 16 + f * 1024, off + 4 * 32 * 8192, AUX_SC1);
      }
      SBAR();
    };
    unsigned so = 0;
    auto compute = [&](int p, const v4u (&B)[16]) {
      so = acquire(36 + p);
      const unsigned sb = so + tro;
#pragma unroll
      for (int hh = 0; hh < 2; hh++) {
#pragma unroll
        for (int i = 0; i < 8; i++) { const unsigned a = (sb ^ (64 * (i & 1))) + (4 * hh + (i >> 1)) * 2048; F[i] = lds_tr2(a, a + 1024); }
        if (hh) { if (p < 7) release(36 + p); repoll(); }
        SBAR();
#pragma unroll
        for (int i = 0; i < 8; i++) {
          acc[2 * (i & 1)] = mfma32(F[i], B[2 * (4 * hh + (i >> 1))], acc[2 * (i & 1)]);
          acc[2 * (i & 1) + 1] = mfma32(F[i], B[2 * (4 * hh + (i >> 1)) + 1], acc[2 * (i & 1) + 1]);
        }
        SBAR();
      }
    };
    ZERO_ACC();
    v4u B0[16], B1[16];
    repoll();
    fetch(0, B0);
#pragma unroll 1
    for (int p = 0; p < 8; p += 2) {
      fetch(p + 1, B1);
      compute(p, B0);
      got = __builtin_amdgcn_readfirstlane(ctrl[6]);
      const bool early = (got >> op_head((p + 2) & 7, w, ob0, ont)) & 1;
#pragma unroll 1
      for (int ph = 0; ph < 2; ph++) {
        if (p < 6 && (ph == 0) == early) fetch(p + 2, B0);
        if (ph == 0) compute(p + 1, B1);
      }
    }
#pragma unroll
    for (int c = 0; c < 4; c++)
#pragma unroll
      for (int g = 0; g < 4; g++) *(LAS f32x4*)(size_t)(so + (c * 4 + g) * 1024 + lane * 16) = f32x4{acc[c][4 * g], acc[c][4 * g + 1], acc[c][4 * g + 2], acc[c][4 * g + 3]};
    release(44);
    {
      unsigned sp = 0;
#pragma unroll 1
      while (((unsigned)__ballot((int)ctrl[8 + (lane & 7)] < 45) & 15u) != 0u) { if (++sp > SPIN_LDS) break; }
      CFENCE();
    }
    f32x4 r[4];
#pragma unroll
    for (int g = 0; g < 4; g++) r[g] = f32x4{0.f, 0.f, 0.f, 0.f};
#pragma unroll
    for (int k = 0; k < 4; k++) {
      const unsigned sl = ((kSchedH.cs[2][k][43] >> 8) & 15) * SLOT + w * 4096 + lane * 16;
#pragma unroll
      for (int g = 0; g < 4; g++) r[g] += *(const LAS f32x4*)(size_t)(sl + g * 1024);
    }
    char* dst = (char*)out + ((size_t)((ob0 + 4 * (w & 1)) * 32 + r32) * 1024 + 512 + ont * 64 + 32 * (w >> 1) + 8 * h) * 2;
    const v4u o0 = {pk(r[0][0], r[0][1]), pk(r[0][2], r[0][3]), pk(r[1][0], r[1][1]), pk(r[1][2], r[1][3])};
    const v4u o1 = {pk(r[2][0], r[2][1]), pk(r[2][2], r[2][3]), pk(r[3][0], r[3][1]), pk(r[3][2], r[3][3])};
    const int oo = (int)(dst - (char*)out);
    __builtin_amdgcn_raw_buffer_store_b128(o0, rout, oo, 0, 0);
    __builtin_amdgcn_raw_buffer_store_b128(o1, rout, oo + 32, 0, 0);
  }
}

extern "C" void kernel_launch(void* const* d_in, const int* in_sizes, int n_in, void* d_out, int out_size, void* d_ws, size_t ws_size, hipStream_t stream) {
  (void)hipFuncSetAttribute((const void*)k_fused, hipFuncAttributeMaxDynamicSharedMemorySize, LDS1);
  k_fused<<<256, 512, LDS1, stream>>>(d_in[0], d_in[1], d_in[2], d_in[3], d_in[4], d_in[5], d_in[6], d_in[7], d_in[8], d_in[9], d_out, d_ws);
}
```
